```python
import torch

def _softmax(x, axis=-1):
    return torch.softmax(x, dim=axis)
def _gelu(x):
    return torch.nn.functional.gelu(x, approximate="tanh")
def _silu(x):
    return torch.nn.functional.silu(x)
def _sigmoid(x):
    return torch.sigmoid(x)
def _layernorm(x, axis=-1, eps=1e-5):
    m = x.mean(dim=axis, keepdim=True)
    v = x.var(dim=axis, keepdim=True, unbiased=False)
    return (x - m) / torch.sqrt(v + eps)
def _scatter_add(tgt, idx, src, axis=0):
    return tgt.clone().index_add_(axis, idx, src)
def _segment_sum(src, seg, n):
    out = torch.zeros((n,) + src.shape[1:], dtype=src.dtype)
    return out.index_add_(0, seg, src)
def _pad(x, pw):
    flat = tuple(v for pair in reversed(pw) for v in pair)
    return torch.nn.functional.pad(x, flat)

kernel_name = 'recurrent_scan__gqa_block_cached__glu_gated_geglu__decode'

def setup_inputs():

    _w = (lambda a: (lambda t: t.to(torch.bfloat16) if t.is_floating_point() else t)(torch.as_tensor(a)))
    def _A_setup():
        B, Sq, D = 1, 1, 576
        Hq, Hkv, Sk, Dh = 32, 16, 65536, 128
        assert Hq % Hkv == 0, (Hq, Hkv)
        return {
            "x": _w((lambda *s: torch.randn(*s, dtype=torch.bfloat16) * 0.4)(B, Sq, D)),
            "k_cache": _w((lambda *s: torch.randn(*s, dtype=torch.bfloat16) * 1.6)(B, Hkv, Sk, Dh)),
            "v_cache": _w((lambda *s: torch.randn(*s, dtype=torch.bfloat16) * 1.6)(B, Hkv, Sk, Dh)),
            "wq": _w((lambda *s: torch.randn(*s, dtype=torch.bfloat16) * 1.6)(D, Hq * Dh) * (D ** -0.5)),
            "wo": _w((lambda *s: torch.randn(*s, dtype=torch.bfloat16) * 1.6)(Hq * Dh, D) * ((Hq * Dh) ** -0.5)),
        }
    _A_in = _A_setup()

    def _B_setup():
        B, S, Dm, Df = 1, 1, 576, 4608
        return {
            "x": _w((lambda *s: torch.randn(*s, dtype=torch.bfloat16) * 0.4)(B, S, Dm)),
            "w_gate": _w((lambda *s: torch.randn(*s, dtype=torch.bfloat16) * 1.6)(Dm, Df) * (Dm ** -0.5)),
            "w_up": _w((lambda *s: torch.randn(*s, dtype=torch.bfloat16) * 1.6)(Dm, Df) * (Dm ** -0.5)),
            "w_down": _w((lambda *s: torch.randn(*s, dtype=torch.bfloat16) * 1.6)(Df, Dm) * (Df ** -0.5)),
        }
    _B_in = _B_setup()

    return {
        "A_x": _A_in["x"],
        "A_k_cache": _A_in["k_cache"],
        "A_v_cache": _A_in["v_cache"],
        "A_wq": _A_in["wq"],
        "A_wo": _A_in["wo"],
        "B_x": _B_in["x"],
        "B_w_gate": _B_in["w_gate"],
        "B_w_up": _B_in["w_up"],
        "B_w_down": _B_in["w_down"],
    }

def reference(A_x, A_k_cache, A_v_cache, A_wq, A_wo, B_x, B_w_gate, B_w_up, B_w_down):
    def _A_ref(x, k_cache, v_cache, wq, wo):
        B, Sq, D = x.shape
        Hq, Dh = 32, 128
        Hkv = k_cache.shape[1]
        rep = Hq // Hkv
        q = (lambda x, *d: x.permute(*d))((x @ wq).reshape(B, Sq, Hq, Dh), 0, 2, 1, 3)
        k = (lambda x, n, axis: torch.repeat_interleave(x, n, dim=axis))(k_cache, rep, axis=1)
        v = (lambda x, n, axis: torch.repeat_interleave(x, n, dim=axis))(v_cache, rep, axis=1)
        scores = torch.einsum("bhqd,bhkd->bhqk", q, k) * (Dh ** -0.5)
        p = _softmax(scores, axis=-1)
        o = torch.einsum("bhqk,bhkd->bhqd", p, v)
        return (lambda x, *d: x.permute(*d))(o, 0, 2, 1, 3).reshape(B, Sq, Hq * Dh) @ wo

    def _B_ref(x, w_gate, w_up, w_down):
        g = torch.einsum("bsd,df->bsf", x, w_gate)
        u = torch.einsum("bsd,df->bsf", x, w_up)
        h = _gelu(g) * u
        return torch.einsum("bsf,fd->bsd", h, w_down)

    x = A_x
    xn = _layernorm(x, axis=-1)
    s = _A_ref(xn, A_k_cache, A_v_cache, A_wq, A_wo)
    g = _silu(_B_ref(xn, B_w_gate, B_w_up, B_w_down))
    return x + s * g
```

```cpp
#include <hip/hip_runtime.h>
#include <hip/hip_bf16.h>
#include <cstdint>

typedef unsigned short u16;
using u32x4 = __attribute__((ext_vector_type(4))) unsigned;
using f32x4 = __attribute__((ext_vector_type(4))) float;
using bf16x2 = __attribute__((ext_vector_type(2))) __bf16;

constexpr int DM = 576, HQ = 32, HKV = 16, SK = 65536, DH = 128, DF = 4608, QD = HQ * DH;
constexpr int NSPLIT = 32;
constexpr int TPS = SK / NSPLIT;

constexpr size_t WS_Q = 0;
constexpr size_t WS_H = 8192;
constexpr size_t WS_ACC = WS_H + DF * 4;
constexpr size_t WS_PML = 65536;
constexpr size_t WS_PO = WS_PML + (size_t)HQ * NSPLIT * 2 * 4 + 1024;

__device__ __forceinline__ float bf2f(u16 v) { return __uint_as_float(((unsigned)v) << 16); }
__device__ __forceinline__ u16 f2bf(float f) {
  unsigned u = __float_as_uint(f);
  u += 0x7FFFu + ((u >> 16) & 1u);
  return (u16)(u >> 16);
}
__device__ __forceinline__ float rbf(float f) { return bf2f(f2bf(f)); }

__device__ __forceinline__ float dot2(unsigned k, unsigned q, float acc) {
  return __builtin_amdgcn_fdot2_f32_bf16(__builtin_bit_cast(bf16x2, k), __builtin_bit_cast(bf16x2, q), acc, false);
}
template <int CTRL> __device__ __forceinline__ float dpp(float x) {
  return __builtin_bit_cast(float, __builtin_amdgcn_mov_dpp(__builtin_bit_cast(int, x), CTRL, 0xf, 0xf, true));
}
constexpr int XOR1 = 0xB1, XOR2 = 0x4E, XOR7 = 0x141, XOR8 = 0x128, ROR4 = 0x124;
__device__ __forceinline__ float xrow16_max(float x) {
  auto s = __builtin_amdgcn_permlane16_swap(__float_as_uint(x), __float_as_uint(x), false, false);
  x = fmaxf(__uint_as_float(s[0]), __uint_as_float(s[1]));
  auto t = __builtin_amdgcn_permlane32_swap(__float_as_uint(x), __float_as_uint(x), false, false);
  return fmaxf(__uint_as_float(t[0]), __uint_as_float(t[1]));
}
__device__ __forceinline__ float xrow16_sum(float x) {
  auto s = __builtin_amdgcn_permlane16_swap(__float_as_uint(x), __float_as_uint(x), false, false);
  x = __uint_as_float(s[0]) + __uint_as_float(s[1]);
  auto t = __builtin_amdgcn_permlane32_swap(__float_as_uint(x), __float_as_uint(x), false, false);
  return __uint_as_float(t[0]) + __uint_as_float(t[1]);
}

__device__ __forceinline__ void layernorm_to_lds(const u16* x, float* xn, float* red) {
  const int tid = threadIdx.x;
  float v0 = bf2f(x[tid]), v1 = bf2f(x[tid + 256]), v2 = tid < 64 ? bf2f(x[tid + 512]) : 0.f;
  float s = v0 + v1 + v2;
  for (int o = 32; o >= 1; o >>= 1) s += __shfl_xor(s, o);
  if ((tid & 63) == 0) red[tid >> 6] = s;
  __syncthreads();
  float mean = (red[0] + red[1] + red[2] + red[3]) * (1.f / DM);
  __syncthreads();
  float d0 = v0 - mean, d1 = v1 - mean, d2 = tid < 64 ? v2 - mean : 0.f;
  float q = d0 * d0 + d1 * d1 + d2 * d2;
  for (int o = 32; o >= 1; o >>= 1) q += __shfl_xor(q, o);
  if ((tid & 63) == 0) red[tid >> 6] = q;
  __syncthreads();
  float var = rbf((red[0] + red[1] + red[2] + red[3]) * (1.f / DM));
  float mb = rbf(mean);
  float sd = rbf(sqrtf(rbf(var + 1e-5f)));
  xn[tid] = rbf(rbf(v0 - mb) / sd);
  xn[tid + 256] = rbf(rbf(v1 - mb) / sd);
  if (tid < 64) xn[tid + 512] = rbf(rbf(v2 - mb) / sd);
  __syncthreads();
}

__device__ __forceinline__ float gelu_tanh(float g) {
  const float k0 = 0.7978845608028654f, k1 = 0.044715f;
  float inner = k0 * (g + k1 * g * g * g);
  return 0.5f * g * (1.f + tanhf(inner));
}

__global__ void __launch_bounds__(256) k_prep(const u16* __restrict__ x, const u16* __restrict__ wq,
                                              const u16* __restrict__ wg, const u16* __restrict__ wu,
                                              unsigned char* ws) {
  __shared__ float xn[DM];
  __shared__ float red[4];
  __shared__ float part[2][4][64];
  const int tid = threadIdx.x, lane = tid & 63, wave = tid >> 6;
  const int b = blockIdx.x;
  if (b == 0) {
    float* accs = (float*)(ws + WS_ACC);
    for (int i = tid; i < 2 * DM + 4; i += 256) accs[i] = 0.f;
  }
  layernorm_to_lds(x, xn, red);
  const bool isq = b < 64;
  const int col0 = isq ? 64 * b : 64 * (b - 64);
  const int ld = isq ? QD : DF;
  const int rr = lane >> 3, cc = lane & 7;
  float a0[8], a1[8];
#pragma unroll
  for (int k = 0; k < 8; ++k) { a0[k] = 0.f; a1[k] = 0.f; }
  const u16* w0 = isq ? wq : wg;
#pragma unroll 6
  for (int i = 0; i < 18; ++i) {
    const int row = wave * 144 + i * 8 + rr;
    const float xv = xn[row];
    u32x4 v = *reinterpret_cast<const u32x4*>(w0 + (size_t)row * ld + col0 + cc * 8);
#pragma unroll
    for (int k = 0; k < 4; ++k) {
      a0[2 * k] = fmaf(xv, __uint_as_float(v[k] << 16), a0[2 * k]);
      a0[2 * k + 1] = fmaf(xv, __uint_as_float(v[k] & 0xffff0000u), a0[2 * k + 1]);
    }
    if (!isq) {
      u32x4 u = *reinterpret_cast<const u32x4*>(wu + (size_t)row * ld + col0 + cc * 8);
#pragma unroll
      for (int k = 0; k < 4; ++k) {
        a1[2 * k] = fmaf(xv, __uint_as_float(u[k] << 16), a1[2 * k]);
        a1[2 * k + 1] = fmaf(xv, __uint_as_float(u[k] & 0xffff0000u), a1[2 * k + 1]);
      }
    }
  }
#pragma unroll
  for (int k = 0; k < 8; ++k) {
    for (int o = 8; o <= 32; o <<= 1) { a0[k] += __shfl_xor(a0[k], o); a1[k] += __shfl_xor(a1[k], o); }
  }
  if (lane < 8) {
#pragma unroll
    for (int k = 0; k < 8; ++k) { part[0][wave][lane * 8 + k] = a0[k]; part[1][wave][lane * 8 + k] = a1[k]; }
  }
  __syncthreads();
  if (tid < 64) {
    float g = part[0][0][tid] + part[0][1][tid] + part[0][2][tid] + part[0][3][tid];
    if (isq) {
      ((u16*)(ws + WS_Q))[col0 + tid] = f2bf(g);
    } else {
      float u = part[1][0][tid] + part[1][1][tid] + part[1][2][tid] + part[1][3][tid];
      g = rbf(g); u = rbf(u);
      float h = rbf(rbf(gelu_tanh(g)) * u);
      ((float*)(ws + WS_H))[col0 + tid] = h;
    }
  }
}

__global__ void __launch_bounds__(256, 2) k_attn(const u16* __restrict__ kc, const u16* __restrict__ vc,
                                                 unsigned char* ws) {
  __shared__ __attribute__((aligned(16))) float lds_p[4][4][16];
  __shared__ __attribute__((aligned(16))) float mrg[4][2][4 + DH];
  const int s = blockIdx.x, kvh = blockIdx.y;
  const int tid = threadIdx.x, lane = tid & 63, wave = __builtin_amdgcn_readfirstlane(tid >> 6);
  const int r = lane >> 4, c = lane & 15;
  constexpr float C2 = 0.08838834764831845f * 1.4426950408889634f;
  const u16* qp = (const u16*)(ws + WS_Q) + (size_t)kvh * 2 * DH + 8 * c;
  u32x4 q0 = *reinterpret_cast<const u32x4*>(qp);
  u32x4 q1 = *reinterpret_cast<const u32x4*>(qp + DH);
  const size_t base = ((size_t)kvh * SK + (size_t)s * TPS + wave * 32 + r) * DH + 8 * c;
  const u16* kp = kc + base;
  const u16* vp = vc + base;
  constexpr int NIT = TPS / 128;
  u32x4 Kn[8], Vn[8];
#pragma unroll
  for (int t = 0; t < 8; ++t) {
    Kn[t] = __builtin_nontemporal_load(reinterpret_cast<const u32x4*>(kp + (size_t)t * 4 * DH));
    Vn[t] = __builtin_nontemporal_load(reinterpret_cast<const u32x4*>(vp + (size_t)t * 4 * DH));
  }
  float m = -INFINITY, l = 0.f;
  float acc[16];
#pragma unroll
  for (int k = 0; k < 16; ++k) acc[k] = 0.f;
  float* prow = &lds_p[wave][r][0];
  for (int it = 0; it < NIT; ++it) {
    u32x4 Kc[8], Vc[8];
#pragma unroll
    for (int t = 0; t < 8; ++t) { Kc[t] = Kn[t]; Vc[t] = Vn[t]; }
    if (it + 1 < NIT) {
      const u16* kq = kp + (size_t)(it + 1) * 128 * DH;
      const u16* vq = vp + (size_t)(it + 1) * 128 * DH;
#pragma unroll
      for (int t = 0; t < 8; ++t) {
        Kn[t] = __builtin_nontemporal_load(reinterpret_cast<const u32x4*>(kq + (size_t)t * 4 * DH));
        Vn[t] = __builtin_nontemporal_load(reinterpret_cast<const u32x4*>(vq + (size_t)t * 4 * DH));
      }
    }
    float x[16];
#pragma unroll
    for (int t = 0; t < 8; ++t) {
      float d0 = 0.f, d1 = 0.f;
#pragma unroll
      for (int k = 0; k < 4; ++k) { d0 = dot2(Kc[t][k], q0[k], d0); d1 = dot2(Kc[t][k], q1[k], d1); }
      x[2 * t] = d0; x[2 * t + 1] = d1;
    }
#define TR_STEP(HALF, CTRL, BIT) _Pragma("unroll") for (int h = 0; h < (HALF); ++h) { \
      const float lo_ = x[h], hi_ = x[h + (HALF)]; const float t1_ = lo_ + dpp<CTRL>(lo_), t2_ = hi_ + dpp<CTRL>(hi_); \
      x[h] = (c & (BIT)) ? t2_ : t1_; }
    TR_STEP(8, XOR8, 8) TR_STEP(4, XOR7, 4) TR_STEP(2, XOR2, 2) TR_STEP(1, XOR1, 1)
#undef TR_STEP
    const float sc = x[0] * C2;
    float pm = sc;
    pm = fmaxf(pm, dpp<XOR2>(pm)); pm = fmaxf(pm, dpp<ROR4>(pm)); pm = fmaxf(pm, dpp<XOR8>(pm));
    pm = xrow16_max(pm);
    const float mn = fmaxf(m, pm);
    const float alpha = __builtin_amdgcn_exp2f(m - mn);
    const float p = __builtin_amdgcn_exp2f(sc - mn);
    l = fmaf(l, alpha, p); m = mn;
    const float alo = dpp<XOR1>(alpha);
    const float al0 = (c & 1) ? alo : alpha, al1 = (c & 1) ? alpha : alo;
#pragma unroll
    for (int k = 0; k < 8; ++k) { acc[k] *= al0; acc[8 + k] *= al1; }
    prow[c] = p;
    asm volatile("s_waitcnt lgkmcnt(0)" ::: "memory");
    f32x4 pr[4];
#pragma unroll
    for (int u = 0; u < 4; ++u) pr[u] = *reinterpret_cast<const f32x4*>(prow + 4 * u);
#pragma unroll
    for (int t = 0; t < 8; ++t) {
      float vf[8];
#pragma unroll
      for (int k = 0; k < 4; ++k) { vf[2 * k] = __uint_as_float(Vc[t][k] << 16); vf[2 * k + 1] = __uint_as_float(Vc[t][k] & 0xffff0000u); }
      const float p0 = pr[t >> 1][(t & 1) * 2], p1 = pr[t >> 1][(t & 1) * 2 + 1];
#pragma unroll
      for (int k = 0; k < 8; ++k) { acc[k] = fmaf(p0, vf[k], acc[k]); acc[8 + k] = fmaf(p1, vf[k], acc[8 + k]); }
    }
  }
#pragma unroll
  for (int k = 0; k < 16; ++k) acc[k] = xrow16_sum(acc[k]);
  {
    float t = l; t += dpp<XOR2>(t); t += dpp<ROR4>(t); t += dpp<XOR8>(t); l = xrow16_sum(t);
  }
  if (r == 0) {
#pragma unroll
    for (int g = 0; g < 2; ++g) {
      float* rec = &mrg[wave][g][0];
      *reinterpret_cast<f32x4*>(rec + 4 + 8 * c) = f32x4{acc[g * 8], acc[g * 8 + 1], acc[g * 8 + 2], acc[g * 8 + 3]};
      *reinterpret_cast<f32x4*>(rec + 8 + 8 * c) = f32x4{acc[g * 8 + 4], acc[g * 8 + 5], acc[g * 8 + 6], acc[g * 8 + 7]};
      if (c == g) { rec[0] = m; rec[1] = l; }
    }
  }
  __syncthreads();
  if (wave < 2) {
    const int g = wave, d0 = 2 * lane;
    float M = fmaxf(fmaxf(mrg[0][g][0], mrg[1][g][0]), fmaxf(mrg[2][g][0], mrg[3][g][0]));
    float L = 0.f, o0 = 0.f, o1 = 0.f;
#pragma unroll
    for (int w = 0; w < 4; ++w) {
      const float wt = __builtin_amdgcn_exp2f(mrg[w][g][0] - M);
      L = fmaf(wt, mrg[w][g][1], L); o0 = fmaf(wt, mrg[w][g][4 + d0], o0); o1 = fmaf(wt, mrg[w][g][5 + d0], o1);
    }
    const int head = kvh * 2 + g;
    float* pml = (float*)(ws + WS_PML) + ((size_t)head * NSPLIT + s) * 2;
    float* po = (float*)(ws + WS_PO) + ((size_t)head * NSPLIT + s) * DH;
    *reinterpret_cast<float2*>(po + d0) = make_float2(o0, o1);
    if (lane == 0) *reinterpret_cast<float2*>(pml) = make_float2(M, L);
  }
}

__global__ void __launch_bounds__(256) k_out(const u16* __restrict__ x, const u16* __restrict__ wo,
                                             const u16* __restrict__ wd, unsigned char* ws, u16* out) {
  __shared__ float vec[32];
  __shared__ float wts[NSPLIT];
  __shared__ float part[3][DM];
  __shared__ int lastflag;
  const int tid = threadIdx.x, b = blockIdx.x;
  const bool isatt = b < 128;
  if (isatt) {
    const int head = b >> 2, d0 = (b & 3) * 32;
    const float* pml = (const float*)(ws + WS_PML) + (size_t)head * NSPLIT * 2;
    const float* po = (const float*)(ws + WS_PO) + (size_t)head * NSPLIT * DH;
    if (tid < 64) {
      float M = -INFINITY;
      for (int s = 0; s < NSPLIT; ++s) M = fmaxf(M, pml[2 * s]);
      float L = 0.f;
      for (int s = 0; s < NSPLIT; ++s) L = fmaf(__builtin_amdgcn_exp2f(pml[2 * s] - M), pml[2 * s + 1], L);
      if (tid < 32) {
        float o = 0.f;
        for (int s = 0; s < NSPLIT; ++s) o = fmaf(__builtin_amdgcn_exp2f(pml[2 * s] - M), po[(size_t)s * DH + d0 + tid], o);
        vec[tid] = rbf(o / L);
      }
    }
  } else {
    if (tid < 32) vec[tid] = ((const float*)(ws + WS_H))[(b - 128) * 32 + tid];
  }
  __syncthreads();
  const u16* w = isatt ? wo + (size_t)b * 32 * DM : wd + (size_t)(b - 128) * 32 * DM;
  if (tid < 216) {
    const int rg = tid / 72, cg = tid % 72;
    float a[8];
#pragma unroll
    for (int k = 0; k < 8; ++k) a[k] = 0.f;
    for (int row = rg; row < 32; row += 3) {
      u32x4 v = *reinterpret_cast<const u32x4*>(w + (size_t)row * DM + cg * 8);
      const float xv = vec[row];
#pragma unroll
      for (int k = 0; k < 4; ++k) {
        a[2 * k] = fmaf(xv, __uint_as_float(v[k] << 16), a[2 * k]);
        a[2 * k + 1] = fmaf(xv, __uint_as_float(v[k] & 0xffff0000u), a[2 * k + 1]);
      }
    }
#pragma unroll
    for (int k = 0; k < 8; ++k) part[rg][cg * 8 + k] = a[k];
  }
  __syncthreads();
  float* accs = (float*)(ws + WS_ACC) + (isatt ? 0 : DM);
  for (int i = tid; i < DM; i += 256) atomicAdd(accs + i, part[0][i] + part[1][i] + part[2][i]);
  __threadfence();
  __syncthreads();
  if (tid == 0) {
    unsigned* cnt = (unsigned*)(ws + WS_ACC) + 2 * DM;
    unsigned old = atomicAdd(cnt, 1u);
    lastflag = (old == gridDim.x - 1);
  }
  __syncthreads();
  if (!lastflag) return;
  __threadfence();
  for (int i = tid; i < DM; i += 256) {
    float sv = __hip_atomic_load((float*)(ws + WS_ACC) + i, __ATOMIC_RELAXED, __HIP_MEMORY_SCOPE_AGENT);
    float yv = __hip_atomic_load((float*)(ws + WS_ACC) + DM + i, __ATOMIC_RELAXED, __HIP_MEMORY_SCOPE_AGENT);
    sv = rbf(sv); yv = rbf(yv);
    float g = rbf(yv / (1.f + __expf(-yv)));
    out[i] = f2bf(bf2f(x[i]) + rbf(sv * g));
  }
}

extern "C" void kernel_launch(void* const* d_in, const int* in_sizes, int n_in,
                              void* d_out, int out_size, void* d_ws, size_t ws_size,
                              hipStream_t stream) {
  const u16* x = (const u16*)d_in[0];
  const u16* kc = (const u16*)d_in[1];
  const u16* vc = (const u16*)d_in[2];
  const u16* wq = (const u16*)d_in[3];
  const u16* wo = (const u16*)d_in[4];
  const u16* wg = (const u16*)d_in[6];
  const u16* wu = (const u16*)d_in[7];
  const u16* wd = (const u16*)d_in[8];
  unsigned char* ws = (unsigned char*)d_ws;
  k_prep<<<136, 256, 0, stream>>>(x, wq, wg, wu, ws);
  k_attn<<<dim3(NSPLIT, HKV), 256, 0, stream>>>(kc, vc, ws);
  k_out<<<272, 256, 0, stream>>>(x, wo, wd, ws, (u16*)d_out);
}
```

```cpp
#include <hip/hip_runtime.h>
#include <hip/hip_bf16.h>
#include <cstdint>

typedef unsigned short u16;
using u32x4 = __attribute__((ext_vector_type(4))) unsigned;
using f32x4 = __attribute__((ext_vector_type(4))) float;
using bf16x2 = __attribute__((ext_vector_type(2))) __bf16;

constexpr int DM = 576, HQ = 32, HKV = 16, SK = 65536, DH = 128, DF = 4608, QD = HQ * DH;
constexpr int NSPLIT = 32;
constexpr int TPS = SK / NSPLIT;

constexpr size_t WS_Q = 0;
constexpr size_t WS_H = 8192;
constexpr size_t WS_ACC = WS_H + DF * 4;
constexpr size_t WS_G = 32768;
constexpr size_t WS_PML = 65536;
constexpr size_t WS_PO = WS_PML + (size_t)HQ * NSPLIT * 2 * 4 + 1024;
constexpr size_t WS_YS = 4u << 20;

__device__ __forceinline__ float bf2f(u16 v) { return __uint_as_float(((unsigned)v) << 16); }
__device__ __forceinline__ u16 f2bf(float f) {
  unsigned u = __float_as_uint(f);
  u += 0x7FFFu + ((u >> 16) & 1u);
  return (u16)(u >> 16);
}
__device__ __forceinline__ float rbf(float f) { return bf2f(f2bf(f)); }

__device__ __forceinline__ float dot2(unsigned k, unsigned q, float acc) {
  return __builtin_amdgcn_fdot2_f32_bf16(__builtin_bit_cast(bf16x2, k), __builtin_bit_cast(bf16x2, q), acc, false);
}
template <int CTRL> __device__ __forceinline__ float dpp(float x) {
  return __builtin_bit_cast(float, __builtin_amdgcn_mov_dpp(__builtin_bit_cast(int, x), CTRL, 0xf, 0xf, true));
}
constexpr int XOR1 = 0xB1, XOR2 = 0x4E, XOR7 = 0x141, XOR8 = 0x128, ROR4 = 0x124;
__device__ __forceinline__ float xrow16_max(float x) {
  auto s = __builtin_amdgcn_permlane16_swap(__float_as_uint(x), __float_as_uint(x), false, false);
  x = fmaxf(__uint_as_float(s[0]), __uint_as_float(s[1]));
  auto t = __builtin_amdgcn_permlane32_swap(__float_as_uint(x), __float_as_uint(x), false, false);
  return fmaxf(__uint_as_float(t[0]), __uint_as_float(t[1]));
}
__device__ __forceinline__ float xrow16_sum(float x) {
  auto s = __builtin_amdgcn_permlane16_swap(__float_as_uint(x), __float_as_uint(x), false, false);
  x = __uint_as_float(s[0]) + __uint_as_float(s[1]);
  auto t = __builtin_amdgcn_permlane32_swap(__float_as_uint(x), __float_as_uint(x), false, false);
  return __uint_as_float(t[0]) + __uint_as_float(t[1]);
}

__device__ __forceinline__ void layernorm_to_lds(const u16* x, float* xn, float* red) {
  const int tid = threadIdx.x;
  float v0 = bf2f(x[tid]), v1 = bf2f(x[tid + 256]), v2 = tid < 64 ? bf2f(x[tid + 512]) : 0.f;
  float s = v0 + v1 + v2;
  for (int o = 32; o >= 1; o >>= 1) s += __shfl_xor(s, o);
  if ((tid & 63) == 0) red[tid >> 6] = s;
  __syncthreads();
  float mean = (red[0] + red[1] + red[2] + red[3]) * (1.f / DM);
  __syncthreads();
  float d0 = v0 - mean, d1 = v1 - mean, d2 = tid < 64 ? v2 - mean : 0.f;
  float q = d0 * d0 + d1 * d1 + d2 * d2;
  for (int o = 32; o >= 1; o >>= 1) q += __shfl_xor(q, o);
  if ((tid & 63) == 0) red[tid >> 6] = q;
  __syncthreads();
  float var = rbf((red[0] + red[1] + red[2] + red[3]) * (1.f / DM));
  float mb = rbf(mean);
  float sd = rbf(sqrtf(rbf(var + 1e-5f)));
  xn[tid] = rbf(rbf(v0 - mb) / sd);
  xn[tid + 256] = rbf(rbf(v1 - mb) / sd);
  if (tid < 64) xn[tid + 512] = rbf(rbf(v2 - mb) / sd);
  __syncthreads();
}

__device__ __forceinline__ float gelu_tanh(float g) {
  const float k0 = 0.7978845608028654f, k1 = 0.044715f;
  float inner = k0 * (g + k1 * g * g * g);
  return 0.5f * g * (1.f + tanhf(inner));
}

__global__ void __launch_bounds__(1024) k_prep(const u16* __restrict__ x, const u16* __restrict__ wq,
                                              const u16* __restrict__ wg, const u16* __restrict__ wu,
                                              const u16* __restrict__ wd, unsigned char* ws) {
  __shared__ float xn[DM];
  __shared__ float red[16];
  __shared__ float part[2][16][64];
  __shared__ float hv[64];
  __shared__ float ypart[14][DM];
  const int tid = threadIdx.x, lane = tid & 63, wave = tid >> 6;
  const int b = blockIdx.x;
  const bool isq = b < 64;
  const int col0 = isq ? 64 * b : 64 * (b - 64);
  const int ld = isq ? QD : DF;
  const int rr = lane >> 3, cc = lane & 7;
  const u16* w0 = isq ? wq : wg;
  u32x4 va[5], vb[5], dd[5];
#pragma unroll
  for (int i = 0; i < 5; ++i) {
    const int rl = i * 8 + rr;
    const int row = wave * 36 + (rl < 36 ? rl : 35);
    va[i] = *reinterpret_cast<const u32x4*>(w0 + (size_t)row * ld + col0 + cc * 8);
    if (!isq) vb[i] = *reinterpret_cast<const u32x4*>(wu + (size_t)row * ld + col0 + cc * 8);
  }
  const int drg = tid / 72, dcg = tid % 72;
  if (!isq) {
#pragma unroll
    for (int i = 0; i < 5; ++i) {
      int row = (drg < 14 ? drg : 0) + 14 * i; row = row < 64 ? row : 63;
      dd[i] = *reinterpret_cast<const u32x4*>(wd + (size_t)(col0 + row) * DM + dcg * 8);
    }
  }
  {
    float v0 = tid < DM ? bf2f(x[tid]) : 0.f, v1 = 0.f;
    float s = v0 + v1;
    for (int o = 32; o >= 1; o >>= 1) s += __shfl_xor(s, o);
    if (lane == 0) red[wave] = s;
    __syncthreads();
    float mean = 0.f;
    for (int i = 0; i < 16; ++i) mean += red[i];
    mean *= (1.f / DM);
    __syncthreads();
    float d0 = tid < DM ? v0 - mean : 0.f, d1 = 0.f;
    float q = d0 * d0 + d1 * d1;
    for (int o = 32; o >= 1; o >>= 1) q += __shfl_xor(q, o);
    if (lane == 0) red[wave] = q;
    __syncthreads();
    float var = 0.f;
    for (int i = 0; i < 16; ++i) var += red[i];
    var = rbf(var * (1.f / DM));
    float mb = rbf(mean);
    float sd = rbf(sqrtf(rbf(var + 1e-5f)));
    if (tid < DM) xn[tid] = rbf(rbf(v0 - mb) / sd);
    __syncthreads();
  }
  float a0[8], a1[8];
#pragma unroll
  for (int k = 0; k < 8; ++k) { a0[k] = 0.f; a1[k] = 0.f; }
#pragma unroll
  for (int i = 0; i < 5; ++i) {
    const int rl = i * 8 + rr;
    const float xv = rl < 36 ? xn[wave * 36 + rl] : 0.f;
#pragma unroll
    for (int k = 0; k < 4; ++k) {
      a0[2 * k] = fmaf(xv, __uint_as_float(va[i][k] << 16), a0[2 * k]);
      a0[2 * k + 1] = fmaf(xv, __uint_as_float(va[i][k] & 0xffff0000u), a0[2 * k + 1]);
    }
    if (!isq) {
#pragma unroll
      for (int k = 0; k < 4; ++k) {
        a1[2 * k] = fmaf(xv, __uint_as_float(vb[i][k] << 16), a1[2 * k]);
        a1[2 * k + 1] = fmaf(xv, __uint_as_float(vb[i][k] & 0xffff0000u), a1[2 * k + 1]);
      }
    }
  }
#pragma unroll
  for (int k = 0; k < 8; ++k) {
    for (int o = 8; o <= 32; o <<= 1) { a0[k] += __shfl_xor(a0[k], o); a1[k] += __shfl_xor(a1[k], o); }
  }
  if (lane < 8) {
#pragma unroll
    for (int k = 0; k < 8; ++k) { part[0][wave][lane * 8 + k] = a0[k]; part[1][wave][lane * 8 + k] = a1[k]; }
  }
  __syncthreads();
  if (tid < 64) {
    float g = 0.f, u = 0.f;
    for (int w = 0; w < 16; ++w) { g += part[0][w][tid]; u += part[1][w][tid]; }
    if (isq) {
      ((u16*)(ws + WS_Q))[col0 + tid] = f2bf(g);
    } else {
      g = rbf(g); u = rbf(u);
      hv[tid] = rbf(rbf(gelu_tanh(g)) * u);
    }
  }
  if (isq) return;
  __syncthreads();
  if (drg < 14) {
    float a[8];
#pragma unroll
    for (int k = 0; k < 8; ++k) a[k] = 0.f;
#pragma unroll
    for (int i = 0; i < 5; ++i) {
      const int row = drg + 14 * i;
      const float hvv = row < 64 ? hv[row] : 0.f;
#pragma unroll
      for (int k = 0; k < 4; ++k) {
        a[2 * k] = fmaf(hvv, __uint_as_float(dd[i][k] << 16), a[2 * k]);
        a[2 * k + 1] = fmaf(hvv, __uint_as_float(dd[i][k] & 0xffff0000u), a[2 * k + 1]);
      }
    }
#pragma unroll
    for (int k = 0; k < 8; ++k) ypart[drg][dcg * 8 + k] = a[k];
  }
  __syncthreads();
  float* slab = (float*)(ws + WS_YS) + (size_t)(b - 64) * DM;
  if (tid < DM) { float t = 0.f; for (int j = 0; j < 14; ++j) t += ypart[j][tid]; slab[tid] = t; }
}

__global__ void __launch_bounds__(256, 2) k_attn(const u16* __restrict__ kc, const u16* __restrict__ vc,
                                                 unsigned char* ws) {
  __shared__ __attribute__((aligned(16))) float lds_p[4][4][16];
  __shared__ __attribute__((aligned(16))) float mrg[4][2][4 + DH];
  const int s = blockIdx.x, kvh = blockIdx.y;
  if (s == NSPLIT) {
    if (kvh == 0) {
      const float* ysl = (const float*)(ws + WS_YS);
      for (int i = threadIdx.x; i < DM; i += 256) {
        float y0 = 0.f, y1 = 0.f, y2 = 0.f, y3 = 0.f;
        for (int j = 0; j < 72; j += 4) { y0 += ysl[j * DM + i]; y1 += ysl[(j + 1) * DM + i]; y2 += ysl[(j + 2) * DM + i]; y3 += ysl[(j + 3) * DM + i]; }
        float yv = rbf((y0 + y1) + (y2 + y3));
        ((float*)(ws + WS_G))[i] = rbf(yv / (1.f + __expf(-yv)));
        ((float*)(ws + WS_ACC))[i] = 0.f;
      }
      if (threadIdx.x == 0) ((unsigned*)(ws + WS_ACC))[2 * DM] = 0u;
    }
    return;
  }
  const int tid = threadIdx.x, lane = tid & 63, wave = __builtin_amdgcn_readfirstlane(tid >> 6);
  const int r = lane >> 4, c = lane & 15;
  constexpr float C2 = 0.08838834764831845f * 1.4426950408889634f;
  const u16* qp = (const u16*)(ws + WS_Q) + (size_t)kvh * 2 * DH + 8 * c;
  u32x4 q0 = *reinterpret_cast<const u32x4*>(qp);
  u32x4 q1 = *reinterpret_cast<const u32x4*>(qp + DH);
  const size_t base = ((size_t)kvh * SK + (size_t)s * TPS + wave * 32 + r) * DH + 8 * c;
  const u16* kp = kc + base;
  const u16* vp = vc + base;
  constexpr int NIT = TPS / 128;
  u32x4 Kn[8], Vn[8];
#pragma unroll
  for (int t = 0; t < 8; ++t) {
    Kn[t] = __builtin_nontemporal_load(reinterpret_cast<const u32x4*>(kp + (size_t)t * 4 * DH));
    Vn[t] = __builtin_nontemporal_load(reinterpret_cast<const u32x4*>(vp + (size_t)t * 4 * DH));
  }
  float m = -INFINITY, l = 0.f;
  float acc[16];
#pragma unroll
  for (int k = 0; k < 16; ++k) acc[k] = 0.f;
  float* prow = &lds_p[wave][r][0];
  for (int it = 0; it < NIT; ++it) {
    u32x4 Kc[8], Vc[8];
#pragma unroll
    for (int t = 0; t < 8; ++t) { Kc[t] = Kn[t]; Vc[t] = Vn[t]; }
    if (it + 1 < NIT) {
      const u16* kq = kp + (size_t)(it + 1) * 128 * DH;
      const u16* vq = vp + (size_t)(it + 1) * 128 * DH;
#pragma unroll
      for (int t = 0; t < 8; ++t) {
        Kn[t] = __builtin_nontemporal_load(reinterpret_cast<const u32x4*>(kq + (size_t)t * 4 * DH));
        Vn[t] = __builtin_nontemporal_load(reinterpret_cast<const u32x4*>(vq + (size_t)t * 4 * DH));
      }
    }
    float x[16];
#pragma unroll
    for (int t = 0; t < 8; ++t) {
      float d0 = 0.f, d1 = 0.f;
#pragma unroll
      for (int k = 0; k < 4; ++k) { d0 = dot2(Kc[t][k], q0[k], d0); d1 = dot2(Kc[t][k], q1[k], d1); }
      x[2 * t] = d0; x[2 * t + 1] = d1;
    }
#define TR_STEP(HALF, CTRL, BIT) _Pragma("unroll") for (int h = 0; h < (HALF); ++h) { \
      const float lo_ = x[h], hi_ = x[h + (HALF)]; const float t1_ = lo_ + dpp<CTRL>(lo_), t2_ = hi_ + dpp<CTRL>(hi_); \
      x[h] = (c & (BIT)) ? t2_ : t1_; }
    TR_STEP(8, XOR8, 8) TR_STEP(4, XOR7, 4) TR_STEP(2, XOR2, 2) TR_STEP(1, XOR1, 1)
#undef TR_STEP
    const float sc = x[0] * C2;
    float pm = sc;
    pm = fmaxf(pm, dpp<XOR2>(pm)); pm = fmaxf(pm, dpp<ROR4>(pm)); pm = fmaxf(pm, dpp<XOR8>(pm));
    pm = xrow16_max(pm);
    const float mn = fmaxf(m, pm);
    const float alpha = __builtin_amdgcn_exp2f(m - mn);
    const float p = __builtin_amdgcn_exp2f(sc - mn);
    l = fmaf(l, alpha, p); m = mn;
    const float alo = dpp<XOR1>(alpha);
    const float al0 = (c & 1) ? alo : alpha, al1 = (c & 1) ? alpha : alo;
#pragma unroll
    for (int k = 0; k < 8; ++k) { acc[k] *= al0; acc[8 + k] *= al1; }
    prow[c] = p;
    asm volatile("s_waitcnt lgkmcnt(0)" ::: "memory");
    f32x4 pr[4];
#pragma unroll
    for (int u = 0; u < 4; ++u) pr[u] = *reinterpret_cast<const f32x4*>(prow + 4 * u);
#pragma unroll
    for (int t = 0; t < 8; ++t) {
      float vf[8];
#pragma unroll
      for (int k = 0; k < 4; ++k) { vf[2 * k] = __uint_as_float(Vc[t][k] << 16); vf[2 * k + 1] = __uint_as_float(Vc[t][k] & 0xffff0000u); }
      const float p0 = pr[t >> 1][(t & 1) * 2], p1 = pr[t >> 1][(t & 1) * 2 + 1];
#pragma unroll
      for (int k = 0; k < 8; ++k) { acc[k] = fmaf(p0, vf[k], acc[k]); acc[8 + k] = fmaf(p1, vf[k], acc[8 + k]); }
    }
  }
#pragma unroll
  for (int k = 0; k < 16; ++k) acc[k] = xrow16_sum(acc[k]);
  {
    float t = l; t += dpp<XOR2>(t); t += dpp<ROR4>(t); t += dpp<XOR8>(t); l = xrow16_sum(t);
  }
  if (r == 0) {
#pragma unroll
    for (int g = 0; g < 2; ++g) {
      float* rec = &mrg[wave][g][0];
      *reinterpret_cast<f32x4*>(rec + 4 + 8 * c) = f32x4{acc[g * 8], acc[g * 8 + 1], acc[g * 8 + 2], acc[g * 8 + 3]};
      *reinterpret_cast<f32x4*>(rec + 8 + 8 * c) = f32x4{acc[g * 8 + 4], acc[g * 8 + 5], acc[g * 8 + 6], acc[g * 8 + 7]};
      if (c == g) { rec[0] = m; rec[1] = l; }
    }
  }
  __syncthreads();
  if (wave < 2) {
    const int g = wave, d0 = 2 * lane;
    float M = fmaxf(fmaxf(mrg[0][g][0], mrg[1][g][0]), fmaxf(mrg[2][g][0], mrg[3][g][0]));
    float L = 0.f, o0 = 0.f, o1 = 0.f;
#pragma unroll
    for (int w = 0; w < 4; ++w) {
      const float wt = __builtin_amdgcn_exp2f(mrg[w][g][0] - M);
      L = fmaf(wt, mrg[w][g][1], L); o0 = fmaf(wt, mrg[w][g][4 + d0], o0); o1 = fmaf(wt, mrg[w][g][5 + d0], o1);
    }
    const int head = kvh * 2 + g;
    float* pml = (float*)(ws + WS_PML) + ((size_t)head * NSPLIT + s) * 2;
    float* po = (float*)(ws + WS_PO) + ((size_t)head * NSPLIT + s) * DH;
    *reinterpret_cast<float2*>(po + d0) = make_float2(o0, o1);
    if (lane == 0) *reinterpret_cast<float2*>(pml) = make_float2(M, L);
  }
}

__global__ void __launch_bounds__(512) k_out(const u16* __restrict__ x, const u16* __restrict__ wo,
                                             unsigned char* ws, u16* out) {
  __shared__ float vec[64];
  __shared__ float part[7][DM];
  __shared__ int lastflag;
  const int tid = threadIdx.x, b = blockIdx.x, lane = tid & 63, wave = tid >> 6;
  const int head = b >> 1, d0 = (b & 1) * 64 + (wave & 3) * 16;
  const int rg = tid / 72, cg = tid % 72;
  u32x4 wv[10];
#pragma unroll
  for (int i = 0; i < 10; ++i) {
    int row = (rg < 7 ? rg : 0) + 7 * i; row = row < 64 ? row : 63;
    wv[i] = *reinterpret_cast<const u32x4*>(wo + ((size_t)b * 64 + row) * DM + cg * 8);
  }
  if (wave < 4) {
    const float* pml = (const float*)(ws + WS_PML) + (size_t)head * NSPLIT * 2;
    const float* po = (const float*)(ws + WS_PO) + (size_t)head * NSPLIT * DH;
    const int s = lane & 31, hf = lane >> 5;
    float2 ml = *reinterpret_cast<const float2*>(pml + 2 * s);
    f32x4 p0 = *reinterpret_cast<const f32x4*>(po + (size_t)s * DH + d0 + hf * 8);
    f32x4 p1 = *reinterpret_cast<const f32x4*>(po + (size_t)s * DH + d0 + hf * 8 + 4);
    float M = ml.x;
    for (int o = 16; o >= 1; o >>= 1) M = fmaxf(M, __shfl_xor(M, o));
    const float wt = __builtin_amdgcn_exp2f(ml.x - M);
    float L = wt * ml.y;
    float o8[8] = {wt * p0[0], wt * p0[1], wt * p0[2], wt * p0[3], wt * p1[0], wt * p1[1], wt * p1[2], wt * p1[3]};
    for (int o = 16; o >= 1; o >>= 1) {
      L += __shfl_xor(L, o);
#pragma unroll
      for (int k = 0; k < 8; ++k) o8[k] += __shfl_xor(o8[k], o);
    }
    if (s == 0) {
#pragma unroll
      for (int k = 0; k < 8; ++k) vec[wave * 16 + hf * 8 + k] = rbf(o8[k] / L);
    }
  }
  __syncthreads();
  if (rg < 7) {
    float a[8];
#pragma unroll
    for (int k = 0; k < 8; ++k) a[k] = 0.f;
#pragma unroll
    for (int i = 0; i < 10; ++i) {
      const int row = rg + 7 * i;
      const float xv = row < 64 ? vec[row] : 0.f;
#pragma unroll
      for (int k = 0; k < 4; ++k) {
        a[2 * k] = fmaf(xv, __uint_as_float(wv[i][k] << 16), a[2 * k]);
        a[2 * k + 1] = fmaf(xv, __uint_as_float(wv[i][k] & 0xffff0000u), a[2 * k + 1]);
      }
    }
#pragma unroll
    for (int k = 0; k < 8; ++k) part[rg][cg * 8 + k] = a[k];
  }
  __syncthreads();
  float* accs = (float*)(ws + WS_ACC);
  for (int i = tid; i < DM; i += 512) atomicAdd(accs + i, ((part[0][i] + part[1][i]) + (part[2][i] + part[3][i])) + ((part[4][i] + part[5][i]) + part[6][i]));
  asm volatile("s_waitcnt vmcnt(0)" ::: "memory");
  __syncthreads();
  if (tid == 0) {
    unsigned* cnt = (unsigned*)(ws + WS_ACC) + 2 * DM;
    unsigned old = __hip_atomic_fetch_add(cnt, 1u, __ATOMIC_RELAXED, __HIP_MEMORY_SCOPE_AGENT);
    lastflag = (old == gridDim.x - 1);
  }
  __syncthreads();
  if (!lastflag) return;
  for (int i = tid; i < DM; i += 512) {
    float sv = __hip_atomic_load((float*)(ws + WS_ACC) + i, __ATOMIC_RELAXED, __HIP_MEMORY_SCOPE_AGENT);
    float g = ((const float*)(ws + WS_G))[i];
    sv = rbf(sv);
    out[i] = f2bf(bf2f(x[i]) + rbf(sv * g));
  }
}

extern "C" void kernel_launch(void* const* d_in, const int* in_sizes, int n_in,
                              void* d_out, int out_size, void* d_ws, size_t ws_size,
                              hipStream_t stream) {
  const u16* x = (const u16*)d_in[0];
  const u16* kc = (const u16*)d_in[1];
  const u16* vc = (const u16*)d_in[2];
  const u16* wq = (const u16*)d_in[3];
  const u16* wo = (const u16*)d_in[4];
  const u16* wg = (const u16*)d_in[6];
  const u16* wu = (const u16*)d_in[7];
  const u16* wd = (const u16*)d_in[8];
  unsigned char* ws = (unsigned char*)d_ws;
  k_prep<<<136, 1024, 0, stream>>>(x, wq, wg, wu, wd, ws);
  k_attn<<<dim3(NSPLIT + 1, HKV), 256, 0, stream>>>(kc, vc, ws);
  k_out<<<64, 512, 0, stream>>>(x, wo, ws, (u16*)d_out);
}
```
